```python
import torch

def _softmax(x, axis=-1):
    return torch.softmax(x, dim=axis)
def _gelu(x):
    return torch.nn.functional.gelu(x, approximate="tanh")
def _silu(x):
    return torch.nn.functional.silu(x)
def _sigmoid(x):
    return torch.sigmoid(x)
def _layernorm(x, axis=-1, eps=1e-5):
    m = x.mean(dim=axis, keepdim=True)
    v = x.var(dim=axis, keepdim=True, unbiased=False)
    return (x - m) / torch.sqrt(v + eps)
def _scatter_add(tgt, idx, src, axis=0):
    return tgt.clone().index_add_(axis, idx, src)
def _segment_sum(src, seg, n):
    out = torch.zeros((n,) + src.shape[1:], dtype=src.dtype)
    return out.index_add_(0, seg, src)
def _pad(x, pw):
    flat = tuple(v for pair in reversed(pw) for v in pair)
    return torch.nn.functional.pad(x, flat)

kernel_name = 'standalone__deepnorm__sampling'

def setup_inputs():

    _w = (lambda a: (lambda t: t.to(torch.bfloat16) if t.is_floating_point() else t)(torch.as_tensor(a)))
    B, S, D = 1024, 8, 512
    return {
        "x": _w((lambda *s: torch.randn(*s, dtype=torch.bfloat16) * 0.05)(B, S, D)),
        "f": _w((lambda *s: torch.randn(*s, dtype=torch.bfloat16) * 0.05)(B, S, D)),
        "g": _w((lambda *s: torch.randn(*s, dtype=torch.bfloat16) * 0.05)(D) + 1.0),
        "b": _w((lambda *s: torch.randn(*s, dtype=torch.bfloat16) * 0.4)(D)),
        "alpha": _w(float(2.0)),
    }

def reference(x, f, g, b, alpha):
    return _layernorm(alpha * x + f, axis=-1) * g + b
```

```cpp
#include <hip/hip_runtime.h>
#include <hip/hip_bf16.h>
#include <stdint.h>

typedef uint32_t u32x4 __attribute__((ext_vector_type(4)));
typedef float f32x2 __attribute__((ext_vector_type(2)));
typedef __bf16 bf16x2 __attribute__((ext_vector_type(2)));

#define WPB 4

template <int CTRL>
__device__ __forceinline__ float dpp_add(float v) {
    int t = __builtin_amdgcn_update_dpp(0, __builtin_bit_cast(int, v), CTRL, 0xf, 0xf, true);
    return v + __builtin_bit_cast(float, t);
}

__device__ __forceinline__ float wave_sum(float v) {
    v = dpp_add<0xB1>(v);
    v = dpp_add<0x4E>(v);
    v = dpp_add<0x141>(v);
    v = dpp_add<0x140>(v);
    float a = __builtin_bit_cast(float, __builtin_amdgcn_readlane(__builtin_bit_cast(int, v), 0));
    float b = __builtin_bit_cast(float, __builtin_amdgcn_readlane(__builtin_bit_cast(int, v), 16));
    float c = __builtin_bit_cast(float, __builtin_amdgcn_readlane(__builtin_bit_cast(int, v), 32));
    float d = __builtin_bit_cast(float, __builtin_amdgcn_readlane(__builtin_bit_cast(int, v), 48));
    return (a + b) + (c + d);
}

__device__ __forceinline__ float lo_bf(uint32_t u) { return __builtin_bit_cast(float, u << 16); }
__device__ __forceinline__ float hi_bf(uint32_t u) { return __builtin_bit_cast(float, u & 0xffff0000u); }
__device__ __forceinline__ uint32_t pack_bf(float a, float b) {
    f32x2 v = {a, b};
    bf16x2 r = __builtin_convertvector(v, bf16x2);
    return __builtin_bit_cast(uint32_t, r);
}

__global__ __launch_bounds__(WPB * 64) void deepnorm512_kernel(
    const u32x4* __restrict__ x, const u32x4* __restrict__ f, const u32x4* __restrict__ g,
    const u32x4* __restrict__ b, const uint16_t* __restrict__ alpha_p, u32x4* __restrict__ out,
    int rows) {
    const int lane = threadIdx.x & 63;
    const int row = blockIdx.x * WPB + (threadIdx.x >> 6);
    if (row >= rows) return;
    const size_t idx = (size_t)row * 64 + lane;
    u32x4 xv = x[idx];
    u32x4 fv = f[idx];
    u32x4 gv = g[lane];
    u32x4 bv = b[lane];
    const float alpha = lo_bf(alpha_p[0]);
    float h[8];
#pragma unroll
    for (int i = 0; i < 4; ++i) {
        h[2 * i] = fmaf(alpha, lo_bf(xv[i]), lo_bf(fv[i]));
        h[2 * i + 1] = fmaf(alpha, hi_bf(xv[i]), hi_bf(fv[i]));
    }
    float s = ((h[0] + h[1]) + (h[2] + h[3])) + ((h[4] + h[5]) + (h[6] + h[7]));
    const float mean = wave_sum(s) * (1.0f / 512.0f);
    float q = 0.f;
#pragma unroll
    for (int i = 0; i < 8; ++i) {
        h[i] -= mean;
        q = fmaf(h[i], h[i], q);
    }
    const float var = wave_sum(q) * (1.0f / 512.0f);
    const float rstd = __builtin_amdgcn_rsqf(var + 1e-5f);
    u32x4 o;
#pragma unroll
    for (int i = 0; i < 4; ++i) {
        float a = fmaf(h[2 * i] * rstd, lo_bf(gv[i]), lo_bf(bv[i]));
        float c = fmaf(h[2 * i + 1] * rstd, hi_bf(gv[i]), hi_bf(bv[i]));
        o[i] = pack_bf(a, c);
    }
    out[idx] = o;
}

extern "C" void kernel_launch(void* const* d_in, const int* in_sizes, int n_in,
                              void* d_out, int out_size, void* d_ws, size_t ws_size,
                              hipStream_t stream) {
    const int D = in_sizes[2];
    const int rows = in_sizes[0] / D;
    deepnorm512_kernel<<<(rows + WPB - 1) / WPB, WPB * 64, 0, stream>>>(
        (const u32x4*)d_in[0], (const u32x4*)d_in[1], (const u32x4*)d_in[2],
        (const u32x4*)d_in[3], (const uint16_t*)d_in[4], (u32x4*)d_out, rows);
}
```

```cpp
#include <hip/hip_runtime.h>
#include <hip/hip_bf16.h>
#include <stdint.h>

typedef uint32_t u32x4 __attribute__((ext_vector_type(4)));
typedef uint32_t u32x2 __attribute__((ext_vector_type(2)));
typedef float f32x2 __attribute__((ext_vector_type(2)));
typedef __bf16 bf16x2 __attribute__((ext_vector_type(2)));

static constexpr int kWaves = 16;

__device__ __forceinline__ float asf(uint32_t u) { return __builtin_bit_cast(float, u); }
__device__ __forceinline__ uint32_t asu(float u) { return __builtin_bit_cast(uint32_t, u); }

template <int CTRL>
__device__ __forceinline__ float dpp_add(float v) {
    int t = __builtin_amdgcn_update_dpp(0, (int)asu(v), CTRL, 0xf, 0xf, true);
    return v + asf((uint32_t)t);
}
__device__ __forceinline__ float read_lane(float v, int l) {
    return asf((uint32_t)__builtin_amdgcn_readlane((int)asu(v), l));
}
__device__ __forceinline__ float pair_sum(float a, float b) {
    u32x2 r = __builtin_amdgcn_permlane32_swap(asu(a), asu(b), false, false);
    float v = asf(r[0]) + asf(r[1]);
    v = dpp_add<0xB1>(v);
    v = dpp_add<0x4E>(v);
    v = dpp_add<0x141>(v);
    v = dpp_add<0x140>(v);
    v = dpp_add<0x142>(v);
    return v;
}
__device__ __forceinline__ f32x2 unpack(uint32_t u) {
    f32x2 r = {asf(u << 16), asf(u & 0xffff0000u)};
    return r;
}
__device__ __forceinline__ uint32_t pack_bf(f32x2 v) {
    return __builtin_bit_cast(uint32_t, __builtin_convertvector(v, bf16x2));
}
__device__ __forceinline__ f32x2 fma2(f32x2 a, f32x2 b, f32x2 c) { return __builtin_elementwise_fma(a, b, c); }
__device__ __forceinline__ f32x2 bc(float a) {
    f32x2 r = {a, a};
    return r;
}

__global__ __launch_bounds__(kWaves * 64) void deepnorm512_kernel(
    const char* __restrict__ x, const char* __restrict__ f, const char* __restrict__ g,
    const char* __restrict__ b, const uint32_t* __restrict__ alpha_word, char* __restrict__ out) {
    const uint32_t voff = blockIdx.x * (kWaves * 1024) + threadIdx.x * 16;
    const uint32_t goff = (threadIdx.x & 63) * 16;
    const u32x4 gv = *(const u32x4*)(g + goff);
    const u32x4 bv = *(const u32x4*)(b + goff);
    __builtin_amdgcn_sched_barrier(0);
    const u32x4 xv = *(const u32x4*)(x + voff);
    const u32x4 fv = *(const u32x4*)(f + voff);
    __builtin_amdgcn_sched_barrier(0);
    const float alpha = asf(alpha_word[0] << 16);
    f32x2 gf[4], bf[4];
#pragma unroll
    for (int i = 0; i < 4; ++i) {
        gf[i] = unpack(gv[i]);
        bf[i] = unpack(bv[i]);
    }
    __builtin_amdgcn_sched_barrier(0);
    f32x2 h[4];
#pragma unroll
    for (int i = 0; i < 4; ++i) h[i] = fma2(bc(alpha), unpack(xv[i]), unpack(fv[i]));
    const float shift = asf((uint32_t)__builtin_amdgcn_readfirstlane((int)asu(h[0][0])));
#pragma unroll
    for (int i = 0; i < 4; ++i) h[i] -= bc(shift);
    const f32x2 s = (h[0] + h[1]) + (h[2] + h[3]);
    const f32x2 q = fma2(h[1], h[1], h[0] * h[0]) + fma2(h[3], h[3], h[2] * h[2]);
    const float t = pair_sum(s[0] + s[1], q[0] + q[1]);
    const float m = read_lane(t, 16) * (1.0f / 512.0f);
    const float e = fmaf(read_lane(t, 48), 1.0f / 512.0f, 1e-5f);
    const float rstd = __builtin_amdgcn_rsqf(fmaf(-m, m, e));
    const float nm = -m * rstd;
    u32x4 o;
#pragma unroll
    for (int i = 0; i < 4; ++i) o[i] = pack_bf(fma2(fma2(h[i], bc(rstd), bc(nm)), gf[i], bf[i]));
    *(u32x4*)(out + voff) = o;
}

__global__ __launch_bounds__(64) void deepnorm_generic_kernel(
    const uint16_t* __restrict__ x, const uint16_t* __restrict__ f, const uint16_t* __restrict__ g,
    const uint16_t* __restrict__ b, const uint16_t* __restrict__ alpha_p, __hip_bfloat16* __restrict__ out,
    int D, int first_row) {
    const size_t base = (size_t)(blockIdx.x + first_row) * D;
    const float alpha = asf((uint32_t)alpha_p[0] << 16);
    float s = 0.f;
    for (int j = threadIdx.x; j < D; j += 64)
        s += fmaf(alpha, asf((uint32_t)x[base + j] << 16), asf((uint32_t)f[base + j] << 16));
    for (int o = 32; o; o >>= 1) s += __shfl_xor(s, o, 64);
    const float mean = s / (float)D;
    float q = 0.f;
    for (int j = threadIdx.x; j < D; j += 64) {
        const float d = fmaf(alpha, asf((uint32_t)x[base + j] << 16), asf((uint32_t)f[base + j] << 16)) - mean;
        q = fmaf(d, d, q);
    }
    for (int o = 32; o; o >>= 1) q += __shfl_xor(q, o, 64);
    const float rstd = __builtin_amdgcn_rsqf(q / (float)D + 1e-5f);
    for (int j = threadIdx.x; j < D; j += 64) {
        const float d = fmaf(alpha, asf((uint32_t)x[base + j] << 16), asf((uint32_t)f[base + j] << 16)) - mean;
        out[base + j] = __float2bfloat16(fmaf(d * rstd, asf((uint32_t)g[j] << 16), asf((uint32_t)b[j] << 16)));
    }
}

extern "C" void kernel_launch(void* const* d_in, const int* in_sizes, int n_in,
                              void* d_out, int out_size, void* d_ws, size_t ws_size,
                              hipStream_t stream) {
    const int D = in_sizes[2];
    const int rows = in_sizes[0] / D;
    const uintptr_t mis = ((uintptr_t)d_in[0] | (uintptr_t)d_in[1] | (uintptr_t)d_in[2] | (uintptr_t)d_in[3] |
                           (uintptr_t)d_out) & 15u;
    int done = 0;
    if (D == 512 && mis == 0 && ((uintptr_t)d_in[4] & 3u) == 0 && rows >= kWaves) {
        done = rows / kWaves * kWaves;
        deepnorm512_kernel<<<rows / kWaves, kWaves * 64, 0, stream>>>(
            (const char*)d_in[0], (const char*)d_in[1], (const char*)d_in[2], (const char*)d_in[3],
            (const uint32_t*)d_in[4], (char*)d_out);
    }
    if (done < rows) {
        deepnorm_generic_kernel<<<rows - done, 64, 0, stream>>>(
            (const uint16_t*)d_in[0], (const uint16_t*)d_in[1], (const uint16_t*)d_in[2],
            (const uint16_t*)d_in[3], (const uint16_t*)d_in[4], (__hip_bfloat16*)d_out, D, done);
    }
}
```

```cpp
#include <hip/hip_runtime.h>
#include <hip/hip_bf16.h>
#include <stdint.h>

static constexpr int kRowsPerBlock = 16;

__global__ __launch_bounds__(kRowsPerBlock * 64) void deepnorm512_kernel(
    const void* x, const void* f, const void* g, const void* b, const void* alpha, void* out) {
    asm volatile(
        "s_load_dwordx8 s[4:11], %0, 0x0\n"
        "v_lshlrev_b32 v1, 4, %2\n"
        "v_lshl_or_b32 v2, %1, 14, v1\n"
        "v_and_b32 v1, 0x3f0, v1\n"
        "s_waitcnt lgkmcnt(0)\n"
        "global_load_dwordx4 v[4:7], v2, s[4:5] nt\n"
        "global_load_dwordx4 v[8:11], v2, s[6:7] nt\n"
        "global_load_dwordx4 v[12:15], v1, s[8:9]\n"
        "global_load_dwordx4 v[16:19], v1, s[10:11]\n"
        "s_load_dwordx4 s[12:15], %0, 0x20\n"
        "s_mov_b32 s19, 0xffff0000\n"
        "v_mov_b32 v52, 0x3b000000\n"
        "v_mov_b32 v53, 0x3727c5ac\n"
        "s_waitcnt lgkmcnt(0)\n"
        "s_load_dword s16, s[12:13], 0x0\n"
        "s_waitcnt lgkmcnt(0)\n"
        "s_lshl_b32 s16, s16, 16\n"
        "s_cmpk_lt_u32 %1, 0x100\n"
        "s_waitcnt vmcnt(3)\n"
        "v_lshlrev_b32 v20, 16, v4\n"
        "v_and_b32 v21, s19, v4\n"
        "v_lshlrev_b32 v22, 16, v5\n"
        "v_and_b32 v23, s19, v5\n"
        "v_lshlrev_b32 v24, 16, v6\n"
        "v_and_b32 v25, s19, v6\n"
        "v_lshlrev_b32 v26, 16, v7\n"
        "v_and_b32 v27, s19, v7\n"
        "s_waitcnt vmcnt(2)\n"
        "v_lshlrev_b32 v28, 16, v8\n"
        "v_and_b32 v29, s19, v8\n"
        "v_lshlrev_b32 v30, 16, v9\n"
        "v_pk_fma_f32 v[20:21], s[16:17], v[20:21], v[28:29] op_sel_hi:[0,1,1]\n"
        "v_and_b32 v31, s19, v9\n"
        "v_lshlrev_b32 v32, 16, v10\n"
        "v_readfirstlane_b32 s20, v20\n"
        "v_and_b32 v33, s19, v10\n"
        "v_pk_fma_f32 v[22:23], s[16:17], v[22:23], v[30:31] op_sel_hi:[0,1,1]\n"
        "v_lshlrev_b32 v34, 16, v11\n"
        "v_and_b32 v35, s19, v11\n"
        "v_pk_fma_f32 v[24:25], s[16:17], v[24:25], v[32:33] op_sel_hi:[0,1,1]\n"
        "v_pk_add_f32 v[20:21], v[20:21], s[20:21] op_sel_hi:[1,0] neg_lo:[0,1] neg_hi:[0,1]\n"
        "v_pk_fma_f32 v[26:27], s[16:17], v[26:27], v[34:35] op_sel_hi:[0,1,1]\n"
        "v_pk_add_f32 v[22:23], v[22:23], s[20:21] op_sel_hi:[1,0] neg_lo:[0,1] neg_hi:[0,1]\n"
        "v_pk_add_f32 v[24:25], v[24:25], s[20:21] op_sel_hi:[1,0] neg_lo:[0,1] neg_hi:[0,1]\n"
        "v_pk_add_f32 v[26:27], v[26:27], s[20:21] op_sel_hi:[1,0] neg_lo:[0,1] neg_hi:[0,1]\n"
        "v_pk_mul_f32 v[32:33], v[20:21], v[20:21]\n"
        "v_pk_add_f32 v[28:29], v[20:21], v[22:23]\n"
        "v_pk_mul_f32 v[34:35], v[24:25], v[24:25]\n"
        "v_pk_add_f32 v[30:31], v[24:25], v[26:27]\n"
        "v_pk_fma_f32 v[32:33], v[22:23], v[22:23], v[32:33]\n"
        "v_pk_fma_f32 v[34:35], v[26:27], v[26:27], v[34:35]\n"
        "v_pk_add_f32 v[28:29], v[28:29], v[30:31]\n"
        "s_waitcnt vmcnt(0)\n"
        "v_pk_add_f32 v[32:33], v[32:33], v[34:35]\n"
        "v_add_f32 v28, v28, v29\n"
        "v_add_f32 v32, v32, v33\n"
        "v_lshlrev_b32 v36, 16, v12\n"
        "v_and_b32 v37, s19, v12\n"
        "v_permlane32_swap_b32 v28, v32\n"
        "v_add_f32 v28, v28, v32\n"
        "v_lshlrev_b32 v38, 16, v13\n"
        "v_and_b32 v39, s19, v13\n"
        "v_add_f32_dpp v28, v28, v28 quad_perm:[1,0,3,2] row_mask:0xf bank_mask:0xf bound_ctrl:1\n"
        "v_lshlrev_b32 v40, 16, v14\n"
        "v_and_b32 v41, s19, v14\n"
        "v_add_f32_dpp v28, v28, v28 quad_perm:[2,3,0,1] row_mask:0xf bank_mask:0xf bound_ctrl:1\n"
        "v_lshlrev_b32 v42, 16, v15\n"
        "v_and_b32 v43, s19, v15\n"
        "v_add_f32_dpp v28, v28, v28 row_half_mirror row_mask:0xf bank_mask:0xf bound_ctrl:1\n"
        "v_lshlrev_b32 v44, 16, v16\n"
        "v_and_b32 v45, s19, v16\n"
        "v_add_f32_dpp v28, v28, v28 row_mirror row_mask:0xf bank_mask:0xf bound_ctrl:1\n"
        "v_lshlrev_b32 v46, 16, v17\n"
        "v_and_b32 v47, s19, v17\n"
        "v_add_f32_dpp v28, v28, v28 row_bcast:15 row_mask:0xf bank_mask:0xf bound_ctrl:1\n"
        "v_lshlrev_b32 v48, 16, v18\n"
        "v_readlane_b32 s20, v28, 16\n"
        "v_readlane_b32 s21, v28, 48\n"
        "v_and_b32 v49, s19, v18\n"
        "v_mul_f32 v29, s20, v52\n"
        "v_fma_f32 v30, s21, v52, v53\n"
        "v_fma_f32 v30, -v29, v29, v30\n"
        "v_lshlrev_b32 v50, 16, v19\n"
        "v_rsq_f32 v30, v30\n"
        "v_and_b32 v51, s19, v19\n"
        "v_mul_f32_e64 v31, -v29, v30\n"
        "v_pk_fma_f32 v[20:21], v[20:21], v[30:31], v[30:31] op_sel:[0,0,1] op_sel_hi:[1,0,1]\n"
        "v_pk_fma_f32 v[22:23], v[22:23], v[30:31], v[30:31] op_sel:[0,0,1] op_sel_hi:[1,0,1]\n"
        "v_pk_fma_f32 v[24:25], v[24:25], v[30:31], v[30:31] op_sel:[0,0,1] op_sel_hi:[1,0,1]\n"
        "v_pk_fma_f32 v[26:27], v[26:27], v[30:31], v[30:31] op_sel:[0,0,1] op_sel_hi:[1,0,1]\n"
        "v_pk_fma_f32 v[20:21], v[20:21], v[36:37], v[44:45]\n"
        "v_pk_fma_f32 v[22:23], v[22:23], v[38:39], v[46:47]\n"
        "v_pk_fma_f32 v[24:25], v[24:25], v[40:41], v[48:49]\n"
        "v_pk_fma_f32 v[26:27], v[26:27], v[42:43], v[50:51]\n"
        "v_cvt_pk_bf16_f32 v28, v20, v21\n"
        "v_cvt_pk_bf16_f32 v29, v22, v23\n"
        "v_cvt_pk_bf16_f32 v30, v24, v25\n"
        "v_cvt_pk_bf16_f32 v31, v26, v27\n"
        "s_cbranch_scc1 .Ldeepnorm512_wt\n"
        "global_store_dwordx4 v2, v[28:31], s[14:15]\n"
        "s_endpgm\n"
        ".Ldeepnorm512_wt:\n"
        "global_store_dwordx4 v2, v[28:31], s[14:15] sc1\n"
        :
        : "s"(__builtin_amdgcn_kernarg_segment_ptr()), "s"(blockIdx.x), "v"(threadIdx.x)
        : "memory", "scc", "s4", "s5", "s6", "s7", "s8", "s9", "s10", "s11", "s12", "s13", "s14",
          "s15", "s16", "s17", "s19", "s20", "s21", "v1", "v2", "v4", "v5", "v6", "v7", "v8", "v9",
          "v10", "v11", "v12", "v13", "v14", "v15", "v16", "v17", "v18", "v19", "v20", "v21", "v22",
          "v23", "v24", "v25", "v26", "v27", "v28", "v29", "v30", "v31", "v32", "v33", "v34", "v35",
          "v36", "v37", "v38", "v39", "v40", "v41", "v42", "v43", "v44", "v45", "v46", "v47", "v48",
          "v49", "v50", "v51", "v52", "v53");
}

__device__ __forceinline__ float bf_to_f(uint16_t u) { return __builtin_bit_cast(float, (uint32_t)u << 16); }

__global__ __launch_bounds__(64) void deepnorm_generic_kernel(
    const uint16_t* x, const uint16_t* f, const uint16_t* g, const uint16_t* b,
    const uint16_t* alpha_p, __hip_bfloat16* out, int D, int first_row) {
    const size_t base = (size_t)(blockIdx.x + first_row) * D;
    const float alpha = bf_to_f(alpha_p[0]);
    float s = 0.f;
    for (int j = threadIdx.x; j < D; j += 64) s += fmaf(alpha, bf_to_f(x[base + j]), bf_to_f(f[base + j]));
    for (int o = 32; o; o >>= 1) s += __shfl_xor(s, o, 64);
    const float mean = s / (float)D;
    float q = 0.f;
    for (int j = threadIdx.x; j < D; j += 64) {
        const float d = fmaf(alpha, bf_to_f(x[base + j]), bf_to_f(f[base + j])) - mean;
        q = fmaf(d, d, q);
    }
    for (int o = 32; o; o >>= 1) q += __shfl_xor(q, o, 64);
    const float rstd = __builtin_amdgcn_rsqf(q / (float)D + 1e-5f);
    for (int j = threadIdx.x; j < D; j += 64) {
        const float d = fmaf(alpha, bf_to_f(x[base + j]), bf_to_f(f[base + j])) - mean;
        out[base + j] = __float2bfloat16(fmaf(d * rstd, bf_to_f(g[j]), bf_to_f(b[j])));
    }
}

extern "C" void kernel_launch(void* const* d_in, const int* in_sizes, int n_in,
                              void* d_out, int out_size, void* d_ws, size_t ws_size,
                              hipStream_t stream) {
    const int D = in_sizes[2];
    const int rows = in_sizes[0] / D;
    const uintptr_t mis = (((uintptr_t)d_in[0] | (uintptr_t)d_in[1] | (uintptr_t)d_in[2] |
                            (uintptr_t)d_in[3] | (uintptr_t)d_out) & 15u) | ((uintptr_t)d_in[4] & 3u);
    int done = 0;
    if (D == 512 && mis == 0 && rows >= kRowsPerBlock) {
        done = rows / kRowsPerBlock * kRowsPerBlock;
        deepnorm512_kernel<<<rows / kRowsPerBlock, kRowsPerBlock * 64, 0, stream>>>(
            d_in[0], d_in[1], d_in[2], d_in[3], d_in[4], d_out);
    }
    if (done < rows) {
        deepnorm_generic_kernel<<<rows - done, 64, 0, stream>>>(
            (const uint16_t*)d_in[0], (const uint16_t*)d_in[1], (const uint16_t*)d_in[2],
            (const uint16_t*)d_in[3], (const uint16_t*)d_in[4], (__hip_bfloat16*)d_out, D, done);
    }
}
```
